```python
import torch

def _softmax(x, axis=-1):
    return torch.softmax(x, dim=axis)

def _gelu(x):
    return torch.nn.functional.gelu(x, approximate='tanh')

def _silu(x):
    return torch.nn.functional.silu(x)

def _sigmoid(x):
    return torch.sigmoid(x)

def _layernorm(x, axis=-1, eps=1e-05):
    m = x.mean(dim=axis, keepdim=True)
    v = x.var(dim=axis, keepdim=True, unbiased=False)
    return (x - m) / torch.sqrt(v + eps)

def _scatter_add(tgt, idx, src, axis=0):
    return tgt.clone().index_add_(axis, idx, src)

def _segment_sum(src, seg, n):
    out = torch.zeros((n,) + src.shape[1:], dtype=src.dtype)
    return out.index_add_(0, seg, src)

def _pad(x, pw):
    flat = tuple((v for pair in reversed(pw) for v in pair))
    return torch.nn.functional.pad(x, flat)
kernel_name = 'recurrent_scan__spectral_fnet__mixer_gmlp__prompt'

def setup_inputs():

    _w = lambda a: (lambda t: t.to(torch.bfloat16) if t.is_floating_point() else t)(torch.as_tensor(a))

    def _A_setup():
        B, S, D = (1, 2048, 512)
        ns = torch.arange(S)[:, None] * torch.arange(S)[None, :]
        nd = torch.arange(D)[:, None] * torch.arange(D)[None, :]
        Ws_re = torch.cos(-2.0 * torch.pi * ns / S).to(float)
        Ws_im = torch.sin(-2.0 * torch.pi * ns / S).to(float)
        Wd_re = torch.cos(-2.0 * torch.pi * nd / D).to(float)
        Wd_im = torch.sin(-2.0 * torch.pi * nd / D).to(float)
        return {'x': _w((lambda *s: torch.randn(*s, dtype=torch.bfloat16) * 0.05)(B, S, D)), 'dft_seq_re': _w(Ws_re), 'dft_seq_im': _w(Ws_im), 'dft_hid_re': _w(Wd_re), 'dft_hid_im': _w(Wd_im)}
    _A_in = _A_setup()

    def _B_setup():
        B, S, D, F = (1, 2048, 512, 1376)
        Fh = F // 2
        return {'x': _w((lambda *s: torch.randn(*s, dtype=torch.bfloat16) * 0.05)(B, S, D)), 'w_in': _w((lambda *s: torch.randn(*s, dtype=torch.bfloat16) * 0.4)(D, 2 * Fh)), 'w_sp': _w(0.01 * (lambda *s: torch.randn(*s, dtype=torch.bfloat16) * 0.4)(S, S)), 'b_sp': _w(1.0 + 0.01 * (lambda *s: torch.randn(*s, dtype=torch.bfloat16) * 0.05)(S)), 'w_out': _w((lambda *s: torch.randn(*s, dtype=torch.bfloat16) * 0.05)(Fh, D))}
    _B_in = _B_setup()
    return {'A_x': _A_in['x'], 'A_dft_seq_re': _A_in['dft_seq_re'], 'A_dft_seq_im': _A_in['dft_seq_im'], 'A_dft_hid_re': _A_in['dft_hid_re'], 'A_dft_hid_im': _A_in['dft_hid_im'], 'B_x': _B_in['x'], 'B_w_in': _B_in['w_in'], 'B_w_sp': _B_in['w_sp'], 'B_b_sp': _B_in['b_sp'], 'B_w_out': _B_in['w_out']}

def reference(A_x, A_dft_seq_re, A_dft_seq_im, A_dft_hid_re, A_dft_hid_im, B_x, B_w_in, B_w_sp, B_b_sp, B_w_out):

    def _A_ref(x, dft_seq_re, dft_seq_im, dft_hid_re, dft_hid_im):
        a = torch.einsum('bsd,ed->bse', x, dft_hid_re)
        b = torch.einsum('bsd,ed->bse', x, dft_hid_im)
        y = torch.einsum('ts,bse->bte', dft_seq_re, a) - torch.einsum('ts,bse->bte', dft_seq_im, b)
        return y

    def _B_ref(x, w_in, w_sp, b_sp, w_out):
        B, S, D = x.shape
        F = w_in.shape[-1]
        Fh = F // 2
        z = _gelu(torch.einsum('bsd,df->bsf', x, w_in))
        z1 = z[..., :Fh].clone()
        z2 = _layernorm(z[..., Fh:], axis=-1)
        g = torch.einsum('ts,bsf->btf', w_sp, z2) + b_sp[None, :, None]
        return torch.einsum('bsf,fd->bsd', z1 * g, w_out)
    x = A_x
    xn = _layernorm(x, axis=-1)
    s = _A_ref(xn, A_dft_seq_re, A_dft_seq_im, A_dft_hid_re, A_dft_hid_im)
    g = _silu(_B_ref(xn, B_w_in, B_w_sp, B_b_sp, B_w_out))
    return x + s * g
```

```cpp
#include <hip/hip_runtime.h>
#include <hip/hip_bf16.h>
typedef unsigned short u16;
__device__ __forceinline__ float bf2f(u16 v){ return __uint_as_float(((unsigned)v)<<16); }
__device__ __forceinline__ u16 f2bf(float f){ __hip_bfloat16 h = __float2bfloat16(f); return *(u16*)&h; }
constexpr int S=2048, D=512, F=1376, FH=688;

__global__ void k_ln(const u16* x, u16* xn){
  int row=blockIdx.x; int t=threadIdx.x;
  float v[8]; float s=0;
  for(int j=0;j<8;j++){ v[j]=bf2f(x[row*D+t*8+j]); s+=v[j]; }
  for(int o=32;o;o>>=1) s+=__shfl_xor(s,o);
  float m=s/D; float q=0;
  for(int j=0;j<8;j++){ float d=v[j]-m; q+=d*d; }
  for(int o=32;o;o>>=1) q+=__shfl_xor(q,o);
  float r=1.0f/sqrtf(q/D+1e-5f);
  for(int j=0;j<8;j++) xn[row*D+t*8+j]=f2bf((v[j]-m)*r);
}
__global__ void k_gemm(const u16* A,int lda,const u16* B,int sbk,int sbn,float* C,int ldc,int M,int N,int K,float alpha,int accum){
  int n=blockIdx.x*16+(threadIdx.x&15), m=blockIdx.y*16+(threadIdx.x>>4);
  if(n>=N||m>=M) return;
  float acc=0;
  for(int k=0;k<K;k++) acc+=bf2f(A[(size_t)m*lda+k])*bf2f(B[(size_t)k*sbk+(size_t)n*sbn]);
  if(accum) C[(size_t)m*ldc+n]+=alpha*acc; else C[(size_t)m*ldc+n]=alpha*acc;
}
__global__ void k_cvt(const float* a,u16* o,int n){ int i=blockIdx.x*256+threadIdx.x; if(i<n) o[i]=f2bf(a[i]); }
__global__ void k_gelu_ln(const float* zpre,u16* z1,u16* z2n){
  int row=blockIdx.x; int t=threadIdx.x;
  __shared__ float sh[FH]; __shared__ float red[256];
  for(int j=t;j<F;j+=256){ float x=bf2f(f2bf(zpre[row*F+j])); float g=0.5f*x*(1.0f+tanhf(0.7978845608f*(x+0.044715f*x*x*x)));
    if(j<FH) z1[row*FH+j]=f2bf(g); else sh[j-FH]=bf2f(f2bf(g)); }
  __syncthreads();
  float s=0; for(int j=t;j<FH;j+=256) s+=sh[j]; red[t]=s; __syncthreads();
  for(int o=128;o;o>>=1){ if(t<o) red[t]+=red[t+o]; __syncthreads(); }
  float m=red[0]/FH; __syncthreads();
  float q=0; for(int j=t;j<FH;j+=256){ float d=sh[j]-m; q+=d*d; } red[t]=q; __syncthreads();
  for(int o=128;o;o>>=1){ if(t<o) red[t]+=red[t+o]; __syncthreads(); }
  float r=1.0f/sqrtf(red[0]/FH+1e-5f);
  for(int j=t;j<FH;j+=256) z2n[row*FH+j]=f2bf((sh[j]-m)*r);
}
__global__ void k_h(const float* g,const u16* bsp,const u16* z1,u16* h){ int i=blockIdx.x*256+threadIdx.x; if(i<S*FH){ int t=i/FH; h[i]=f2bf(bf2f(z1[i])*(g[i]+bf2f(bsp[t]))); } }
__global__ void k_final(const u16* x,const float* y,const float* b,u16* o){ int i=blockIdx.x*256+threadIdx.x; if(i<S*D){ float bb=b[i]; float sl=bb/(1.0f+__expf(-bb)); o[i]=f2bf(bf2f(x[i])+y[i]*sl);} }

extern "C" void kernel_launch(void* const* d_in, const int* in_sizes, int n_in, void* d_out, int out_size, void* d_ws, size_t ws_size, hipStream_t stream) {
  const u16* x=(const u16*)d_in[0]; const u16* wsr=(const u16*)d_in[1]; const u16* wsi=(const u16*)d_in[2];
  const u16* wdr=(const u16*)d_in[3]; const u16* wdi=(const u16*)d_in[4]; const u16* win=(const u16*)d_in[6];
  const u16* wsp=(const u16*)d_in[7]; const u16* bsp=(const u16*)d_in[8]; const u16* wout=(const u16*)d_in[9];
  char* w=(char*)d_ws; size_t o=0; auto al=[&](size_t b){ char* p=w+o; o+=(b+255)/256*256; return p; };
  u16* xn=(u16*)al(S*D*2); float* t0=(float*)al((size_t)S*F*4); u16* a=(u16*)al(S*D*2); u16* b=(u16*)al(S*D*2);
  float* y=(float*)al(S*D*4); u16* z1=(u16*)al(S*FH*2); u16* z2n=(u16*)al(S*FH*2); u16* h=(u16*)al(S*FH*2); float* bb=(float*)al(S*D*4);
  k_ln<<<S,64,0,stream>>>(x,xn);
  k_gemm<<<dim3(D/16,S/16),256,0,stream>>>(xn,D,wdr,1,D,t0,D,S,D,D,1.f,0); k_cvt<<<S*D/256,256,0,stream>>>(t0,a,S*D);
  k_gemm<<<dim3(D/16,S/16),256,0,stream>>>(xn,D,wdi,1,D,t0,D,S,D,D,1.f,0); k_cvt<<<S*D/256,256,0,stream>>>(t0,b,S*D);
  k_gemm<<<dim3(D/16,S/16),256,0,stream>>>(wsr,S,a,D,1,y,D,S,D,S,1.f,0);
  k_gemm<<<dim3(D/16,S/16),256,0,stream>>>(wsi,S,b,D,1,y,D,S,D,S,-1.f,1);
  k_gemm<<<dim3(F/16,S/16),256,0,stream>>>(xn,D,win,F,1,t0,F,S,F,D,1.f,0);
  k_gelu_ln<<<S,256,0,stream>>>(t0,z1,z2n);
  k_gemm<<<dim3(FH/16,S/16),256,0,stream>>>(wsp,S,z2n,FH,1,t0,FH,S,FH,S,1.f,0);
  k_h<<<(S*FH+255)/256,256,0,stream>>>(t0,bsp,z1,h);
  k_gemm<<<dim3(D/16,S/16),256,0,stream>>>(h,FH,wout,D,1,bb,D,S,D,FH,1.f,0);
  k_final<<<S*D/256,256,0,stream>>>(x,y,bb,(u16*)d_out);
}
```

```cpp
#include <hip/hip_runtime.h>
#include <hip/hip_bf16.h>

typedef unsigned short u16;
typedef __attribute__((ext_vector_type(8))) __bf16 bf16x8;
typedef __attribute__((ext_vector_type(2))) __bf16 bf16x2;
typedef __attribute__((ext_vector_type(4))) short s16x4;
typedef __attribute__((ext_vector_type(8))) short s16x8;
typedef __attribute__((ext_vector_type(4))) float f32x4;
typedef __attribute__((ext_vector_type(2))) float f32x2;
typedef __attribute__((ext_vector_type(4))) unsigned u32x4;
typedef __attribute__((ext_vector_type(2))) unsigned u32x2;
#define LDS3 __attribute__((address_space(3)))
typedef __amdgpu_buffer_rsrc_t srd_t;

constexpr int S = 2048, D = 512, F = 1376, FH = 688;
constexpr int HP = 704;
constexpr unsigned PANEL = S * 64;

constexpr size_t WS_AB  = 0;
constexpr size_t WS_Z2  = WS_AB + 32 * (size_t)PANEL;
constexpr size_t WS_Z2N = WS_Z2 + 24 * (size_t)PANEL;
constexpr size_t WS_Z1  = WS_Z2N + 24 * (size_t)PANEL;
constexpr size_t WS_H   = WS_Z1 + (size_t)S * FH * 2;
constexpr size_t WS_Y   = WS_H + (size_t)S * HP * 2;

__device__ __forceinline__ float bflo(unsigned v) { return __uint_as_float(v << 16); }
__device__ __forceinline__ float bfhi(unsigned v) { return __uint_as_float(v & 0xffff0000u); }
__device__ __forceinline__ unsigned cvtpk(float lo, float hi) {
  return __builtin_bit_cast(unsigned, __builtin_convertvector(f32x2{lo, hi}, bf16x2));
}
__device__ __forceinline__ srd_t mksrd(const void* p) {
  return __builtin_amdgcn_make_buffer_rsrc((void*)p, (short)0, 0x7fffffff, 0x00020000);
}
__device__ __forceinline__ void dma(unsigned m0v, unsigned voff, srd_t srd, unsigned soff) {
  asm volatile("s_mov_b32 m0, %0\n\ts_nop 0\n\tbuffer_load_dwordx4 %1, %2, %3 offen lds"
               :: "s"(m0v), "v"(voff), "s"(srd), "s"(soff) : "memory");
}
template <int N> __device__ __forceinline__ void vmcnt() { asm volatile("s_waitcnt vmcnt(%0)" :: "n"(N) : "memory"); }
__device__ __forceinline__ void lgkm0() { __builtin_amdgcn_s_waitcnt(0xC07F); }
#define BAR() do { __builtin_amdgcn_sched_barrier(0); __builtin_amdgcn_s_barrier(); __builtin_amdgcn_sched_barrier(0); } while (0)

template <int NPW, int NST, int NK, class Issue>
__device__ __forceinline__ void producer_loop(Issue issue) {
#pragma unroll
  for (int s = 0; s < NST; s++) issue(s, s);
  vmcnt<NPW * (NST - 1)>();
  BAR();
  int slot = 0;
  for (int i = 0; i < NK - NST; i++) {
    vmcnt<NPW * (NST - 2)>();
    BAR();
    issue(slot, i + NST);
    slot = (slot + 1 == NST) ? 0 : slot + 1;
  }
  if constexpr (NST >= 2) { vmcnt<NPW * (NST - 2)>(); BAR(); }
  if constexpr (NST >= 3) { vmcnt<NPW * (NST >= 3 ? NST - 3 : 0)>(); BAR(); }
  if constexpr (NST >= 4) { vmcnt<NPW * (NST >= 4 ? NST - 4 : 0)>(); BAR(); }
  if constexpr (NST >= 5) { vmcnt<NPW * (NST >= 5 ? NST - 5 : 0)>(); BAR(); }
  if constexpr (NST >= 6) { vmcnt<NPW * (NST >= 6 ? NST - 6 : 0)>(); BAR(); }
}

template <int MB, int NB> struct Frags { bf16x8 a[MB]; bf16x8 b[NB]; };
struct Addr { unsigned a, b0, b1, bs; };

__device__ __forceinline__ bf16x8 lds128(unsigned addr) { return *(const LDS3 bf16x8*)(size_t)addr; }
__device__ __forceinline__ bf16x8 ldstr(unsigned addr) {
  s16x4 lo = __builtin_bit_cast(s16x4, __builtin_amdgcn_ds_read_tr16_b64_v4i16((LDS3 s16x4*)(size_t)addr));
  s16x4 hi = __builtin_bit_cast(s16x4, __builtin_amdgcn_ds_read_tr16_b64_v4i16((LDS3 s16x4*)(size_t)(addr + 256)));
  s16x8 v = {lo[0], lo[1], lo[2], lo[3], hi[0], hi[1], hi[2], hi[3]};
  return __builtin_bit_cast(bf16x8, v);
}
template <int MB, int NB, int BT>
__device__ __forceinline__ void rd(Frags<MB, NB>& f, const Addr& ad, unsigned so, int ks) {
  const unsigned a = (ks ? (ad.a ^ 64) : ad.a) + so;
#pragma unroll
  for (int m = 0; m < MB; m++) f.a[m] = lds128(a + m * 2048);
  if constexpr (BT == 0) {
    const unsigned b0 = (ks ? (ad.b0 ^ 64) : ad.b0) + so;
#pragma unroll
    for (int c = 0; c < (NB & ~1); c++) f.b[c] = lds128(b0 + c * 2048);
    if constexpr (NB & 1) f.b[NB - 1] = lds128((ks ? (ad.bs ^ 64) : ad.bs) + so);
  } else {
    const unsigned b0 = ad.b0 + so + ks * 2048, b1 = ad.b1 + so + ks * 2048;
#pragma unroll
    for (int c = 0; c < (NB & ~1); c++) f.b[c] = ldstr(((c & 1) ? b1 : b0) + (c >> 1) * 4096);
    if constexpr (NB & 1) f.b[NB - 1] = ldstr(ad.bs + so + ks * 2048);
  }
}
template <int MB, int NB>
__device__ __forceinline__ void mm(f32x4 (&acc)[MB][NB], const Frags<MB, NB>& f) {
#pragma unroll
  for (int m = 0; m < MB; m++)
#pragma unroll
    for (int c = 0; c < NB; c++) acc[m][c] = __builtin_amdgcn_mfma_f32_16x16x32_bf16(f.b[c], f.a[m], acc[m][c], 0, 0, 0);
}
template <int MB, int NB, int BT> __device__ __forceinline__ void interleave() {
  constexpr int NR = MB + (BT ? 2 * NB : NB), NM = MB * NB;
  constexpr int NI = NR < NM ? NR : NM;
#pragma unroll
  for (int i = 0; i < NI; i++) { __builtin_amdgcn_sched_group_barrier(0x8, 1, 0); __builtin_amdgcn_sched_group_barrier(0x100, 1, 0); }
  if constexpr (NR > NI) __builtin_amdgcn_sched_group_barrier(0x100, NR - NI, 0);
  if constexpr (NM > NI) __builtin_amdgcn_sched_group_barrier(0x8, NM - NI, 0);
}
template <int NB, int BT>
__device__ __forceinline__ Addr mkaddr(unsigned abase, unsigned bbase, int wn, int lane) {
  const int g = lane >> 4, r = lane & 15, q = r >> 2, p = lane & 3;
  constexpr int NPAIR = NB / 2;
  Addr ad;
  ad.a = abase + r * 128 + ((g ^ (r & 7)) * 16);
  if constexpr (BT == 0) {
    const unsigned l = r * 128 + ((g ^ (r & 7)) * 16);
    ad.b0 = bbase + wn * NPAIR * 4096 + l;
    ad.b1 = 0;
    ad.bs = bbase + 2 * NPAIR * 4096 + wn * 2048 + l;
  } else {
    const unsigned l = (8 * g + q) * 64 + 8 * (p & 1);
    const unsigned c0 = (((p >> 1)) ^ ((g & 1) << 1)) * 16, c1 = ((2 + (p >> 1)) ^ ((g & 1) << 1)) * 16;
    ad.b0 = bbase + wn * NPAIR * 4096 + l + c0;
    ad.b1 = bbase + wn * NPAIR * 4096 + l + c1;
    ad.bs = bbase + 2 * NPAIR * 4096 + l + (wn ? c1 : c0);
  }
  return ad;
}
struct NoExtra { template <class Fr> __device__ __forceinline__ void operator()(const Fr&, int) const {} };

template <int MB, int NB, int BT, int NST, int NK, int STAGE, class Extra>
__device__ __forceinline__ void consumer_loop(f32x4 (&acc)[MB][NB], const Addr& ad, Extra extra) {
  Frags<MB, NB> f0, f1;
#pragma unroll
  for (int m = 0; m < MB; m++)
#pragma unroll
    for (int c = 0; c < NB; c++) acc[m][c] = f32x4{0, 0, 0, 0};
  BAR();
  rd<MB, NB, BT>(f0, ad, 0, 0);
  unsigned so = 0;
  for (int i = 0; i < NK - 1; i++) {
    rd<MB, NB, BT>(f1, ad, so, 1);
    mm<MB, NB>(acc, f0);
    interleave<MB, NB, BT>();
    __builtin_amdgcn_sched_barrier(0);
    extra(f0, 0);
    __builtin_amdgcn_sched_barrier(0);
    lgkm0();
    BAR();
    so = (so + STAGE == NST * STAGE) ? 0 : so + STAGE;
    rd<MB, NB, BT>(f0, ad, so, 0);
    mm<MB, NB>(acc, f1);
    interleave<MB, NB, BT>();
    __builtin_amdgcn_sched_barrier(0);
    extra(f1, 1);
    __builtin_amdgcn_sched_barrier(0);
  }
  rd<MB, NB, BT>(f1, ad, so, 1);
  mm<MB, NB>(acc, f0);
  extra(f0, 0);
  mm<MB, NB>(acc, f1);
  extra(f1, 1);
}
__device__ __forceinline__ u32x4 pack_pair(const f32x4& x, const f32x4& y) {
  unsigned x0 = cvtpk(x[0], x[1]), x1 = cvtpk(x[2], x[3]), y0 = cvtpk(y[0], y[1]), y1 = cvtpk(y[2], y[3]);
  auto s0 = __builtin_amdgcn_permlane16_swap(x0, y0, false, false);
  auto s1 = __builtin_amdgcn_permlane16_swap(x1, y1, false, false);
  return u32x4{s0[0], s1[0], s0[1], s1[1]};
}
__device__ __forceinline__ void unswap_pair(const u32x4& v, unsigned& x0, unsigned& x1, unsigned& y0, unsigned& y1) {
  auto s0 = __builtin_amdgcn_permlane16_swap(v[0], v[2], false, false);
  auto s1 = __builtin_amdgcn_permlane16_swap(v[1], v[3], false, false);
  x0 = s0[0]; y0 = s0[1]; x1 = s1[0]; y1 = s1[1];
}
__device__ __forceinline__ float gelu(float v) {
  const float c0 = -2.0f * 0.7978845608028654f * 1.4426950408889634f, c1 = c0 * 0.044715f;
  float e = __builtin_amdgcn_exp2f(v * __builtin_fmaf(v * v, c1, c0));
  return v * __builtin_amdgcn_rcpf(1.0f + e);
}
__device__ __forceinline__ float silu(float v) {
  float e = __builtin_amdgcn_exp2f(v * -1.4426950408889634f);
  return v * __builtin_amdgcn_rcpf(1.0f + e);
}

constexpr int K1_NST = 3, K1_STAGE = 16384 + 20480, K1_NK = 8;
constexpr int K1_X = K1_NST * K1_STAGE;
constexpr int K1_LDS = K1_X + 4096;

template <int NB, int BT>
__device__ __forceinline__ void k1_tile(const u16* x, const u16* wdr, const u16* wdi, const u16* win, char* ws,
                                        unsigned ldsb, int wave, int lane, int mt, int pan0, int rot) {
  constexpr int NPAN = NB;
  if (wave >= 4) {
    const int w = wave - 4;
    const srd_t sx = mksrd(x);
    const unsigned vk = (lane >> 3) * 1024 + (((lane & 7) ^ (lane >> 3)) * 16);
    const unsigned sxo = (mt * 128 + w * 32) * 1024;
    if constexpr (BT == 0) {
      srd_t sw[NPAN]; unsigned so[NPAN];
#pragma unroll
      for (int i = 0; i < NPAN; i++) {
        const int n = pan0 * 32 + (w * NPAN + i) * 8;
        sw[i] = mksrd(n < 512 ? wdr : wdi);
        so[i] = (n & 511) * 1024;
      }
      producer_loop<4 + NPAN, K1_NST, K1_NK>([&](int slot, int step) {
        const unsigned kk = ((step + rot) & 7) * 128;
        const unsigned l = ldsb + slot * K1_STAGE;
#pragma unroll
        for (int i = 0; i < 4; i++) dma(l + w * 4096 + i * 1024, vk, sx, sxo + i * 8192 + kk);
#pragma unroll
        for (int i = 0; i < NPAN; i++) dma(l + 16384 + (w * NPAN + i) * 1024, vk, sw[i], so[i] + kk);
      });
    } else {
      const srd_t sw = mksrd(win);
      const int row = lane >> 2, j = (lane & 3) ^ (((row >> 3) & 1) << 1);
      const unsigned vt = row * (F * 2) + j * 16;
      const unsigned so = pan0 * 64 + w * 16 * (F * 2);
      producer_loop<4 + NPAN, K1_NST, K1_NK>([&](int slot, int step) {
        const unsigned k = (step + rot) & 7;
        const unsigned l = ldsb + slot * K1_STAGE;
#pragma unroll
        for (int i = 0; i < 4; i++) dma(l + w * 4096 + i * 1024, vk, sx, sxo + i * 8192 + k * 128);
#pragma unroll
        for (int i = 0; i < NPAN; i++) dma(l + 16384 + i * 4096 + w * 1024, vt, sw, so + i * 64 + k * (64 * F * 2));
      });
    }
    BAR();
    return;
  }
  const int wm = wave >> 1, wn = wave & 1;
  const int g = lane >> 4, r = lane & 15;
  f32x4 acc[4][NB], rs[4], sq[4], cs[NB];
  const unsigned one2 = 0x3F803F80u;
  const bf16x8 ones = __builtin_bit_cast(bf16x8, u32x4{one2, one2, one2, one2});
#pragma unroll
  for (int m = 0; m < 4; m++) { rs[m] = f32x4{0, 0, 0, 0}; sq[m] = f32x4{0, 0, 0, 0}; }
#pragma unroll
  for (int c = 0; c < NB; c++) cs[c] = f32x4{0, 0, 0, 0};
  const Addr ad = mkaddr<NB, BT>(ldsb + wm * 8192, ldsb + 16384, wn, lane);
  consumer_loop<4, NB, BT, K1_NST, K1_NK, K1_STAGE>(acc, ad, [&](const Frags<4, NB>& f, int ks) {
    if (wm == ks) {
#pragma unroll
      for (int c = 0; c < NB; c++) cs[c] = __builtin_amdgcn_mfma_f32_16x16x32_bf16(f.b[c], ones, cs[c], 0, 0, 0);
    }
    if (wn == ks) {
#pragma unroll
      for (int m = 0; m < 4; m++) {
        rs[m] = __builtin_amdgcn_mfma_f32_16x16x32_bf16(ones, f.a[m], rs[m], 0, 0, 0);
        sq[m] = __builtin_amdgcn_mfma_f32_16x16x32_bf16(f.a[m], f.a[m], sq[m], 0, 0, 0);
      }
    }
  });
  LDS3 float* xr = (LDS3 float*)(size_t)(ldsb + K1_X);
  LDS3 float* xq = xr + 256;
  LDS3 float* xc = xq + 256;
#pragma unroll
  for (int m = 0; m < 4; m++) {
    if (g == 0) xr[wave * 64 + m * 16 + r] = rs[m][0];
    const int e = r & 3;
    const float d = e == 0 ? sq[m][0] : e == 1 ? sq[m][1] : e == 2 ? sq[m][2] : sq[m][3];
    if (g == (r >> 2)) xq[wave * 64 + m * 16 + r] = d;
  }
  if (r == 0) {
#pragma unroll
    for (int c = 0; c < NB; c++) *(LDS3 f32x4*)(xc + wave * 80 + c * 16 + g * 4) = cs[c];
  }
  lgkm0();
  BAR();
  float rr[4], mr[4];
#pragma unroll
  for (int m = 0; m < 4; m++) {
    const int i = m * 16 + r;
    const float sum = xr[wave * 64 + i] + xr[(wave ^ 1) * 64 + i];
    const float ssq = xq[wave * 64 + i] + xq[(wave ^ 1) * 64 + i];
    const float mean = sum * (1.0f / D);
    const float var = fmaxf(ssq * (1.0f / D) - mean * mean, 0.0f);
    rr[m] = __builtin_amdgcn_rsqf(var + 1e-5f);
    mr[m] = mean;
  }
#pragma unroll
  for (int c = 0; c < NB; c++) cs[c] += *(LDS3 f32x4*)(xc + (wave ^ 2) * 80 + c * 16 + g * 4);

  constexpr int NPAIR = NB / 2;
  const int jch = (g & 1) * 2 + (g >> 1);
  const unsigned swz = ((r >> 3) & 1) << 1;
#pragma unroll
  for (int m = 0; m < 4; m++) {
    const int t = mt * 128 + wm * 64 + m * 16 + r;
#pragma unroll
    for (int c = 0; c < NB; c++) {
#pragma unroll
      for (int e = 0; e < 4; e++) {
        float v = rr[m] * __builtin_fmaf(-mr[m], cs[c][e], acc[m][c][e]);
        acc[m][c][e] = BT ? gelu(v) : v;
      }
    }
#pragma unroll
    for (int pr = 0; pr < NPAIR; pr++) {
      const u32x4 v = pack_pair(acc[m][2 * pr], acc[m][2 * pr + 1]);
      const int P = pan0 + wn * NPAIR + pr;
      char* dst;
      if constexpr (BT == 0) dst = ws + WS_AB + (size_t)P * PANEL + t * 64 + ((jch ^ swz) * 16);
      else {
        const int col = P * 32 + jch * 8;
        dst = col < FH ? ws + WS_Z1 + (size_t)t * (FH * 2) + col * 2
                       : ws + WS_Z2 + (size_t)(P - 21) * PANEL + t * 64 + ((jch ^ swz) * 16);
      }
      *(u32x4*)dst = v;
    }
    if constexpr (NB & 1) {
      const f32x4& a = acc[m][NB - 1];
      const u32x2 v = {cvtpk(a[0], a[1]), cvtpk(a[2], a[3])};
      const int P = pan0 + 2 * NPAIR;
      const int j = wn * 2 + (g >> 1);
      char* dst;
      if constexpr (BT == 0) dst = ws + WS_AB + (size_t)P * PANEL + t * 64 + ((j ^ swz) * 16) + (g & 1) * 8;
      else {
        const int col = P * 32 + wn * 16 + g * 4;
        dst = col < FH ? ws + WS_Z1 + (size_t)t * (FH * 2) + col * 2
                       : ws + WS_Z2 + (size_t)(P - 21) * PANEL + t * 64 + ((j ^ swz) * 16) + (g & 1) * 8;
      }
      *(u32x2*)dst = v;
    }
  }
}

__global__ __launch_bounds__(512, 2) void k1(const u16* __restrict__ x, const u16* __restrict__ wdr, const u16* __restrict__ wdi,
                                             const u16* __restrict__ win, char* __restrict__ ws) {
  extern __shared__ __attribute__((aligned(16))) char lds[];
  const unsigned ldsb = (unsigned)(size_t)lds;
  const int lane = threadIdx.x & 63, wave = __builtin_amdgcn_readfirstlane(threadIdx.x >> 6);
  const int xcd = blockIdx.x & 7, idx = blockIdx.x >> 3;
  const int mt = (xcd & 1) * 8 + (idx & 7), nt = (xcd >> 1) * 4 + (idx >> 3);
  const int rot = ((idx >> 3) * 2 + (idx & 7)) & 7;
  if (nt < 7) {
    if (nt < 4) k1_tile<5, 0>(x, wdr, wdi, win, ws, ldsb, wave, lane, mt, nt * 5, rot);
    else k1_tile<4, 0>(x, wdr, wdi, win, ws, ldsb, wave, lane, mt, 20 + (nt - 4) * 4, rot);
  } else {
    const int z = nt - 7;
    if (z < 7) k1_tile<5, 1>(x, wdr, wdi, win, ws, ldsb, wave, lane, mt, z * 5, rot);
    else k1_tile<4, 1>(x, wdr, wdi, win, ws, ldsb, wave, lane, mt, 35 + (z - 7) * 4, rot);
  }
}

__global__ __launch_bounds__(256) void k1b(char* __restrict__ ws) {
  const int s = blockIdx.x * 8 + (threadIdx.x >> 5), l = threadIdx.x & 31;
  const unsigned swz = ((s >> 3) & 1) << 1;
  u32x4 v[3]; bool ok[3];
  float sum = 0.f;
#pragma unroll
  for (int i = 0; i < 3; i++) {
    const int ch = l + 32 * i, P = ch >> 2, jp = ch & 3;
    const int col = P * 32 + (jp ^ swz) * 8;
    ok[i] = col >= 16 && col < FH + 16;
    v[i] = u32x4{0, 0, 0, 0};
    if (ok[i]) v[i] = *(const u32x4*)(ws + WS_Z2 + (size_t)P * PANEL + s * 64 + jp * 16);
#pragma unroll
    for (int e = 0; e < 4; e++) sum += bflo(v[i][e]) + bfhi(v[i][e]);
  }
  for (int o = 16; o; o >>= 1) sum += __shfl_xor(sum, o);
  const float mean = sum * (1.0f / FH);
  float q = 0.f;
#pragma unroll
  for (int i = 0; i < 3; i++)
    if (ok[i]) {
#pragma unroll
      for (int e = 0; e < 4; e++) { float a = bflo(v[i][e]) - mean, b = bfhi(v[i][e]) - mean; q += a * a + b * b; }
    }
  for (int o = 16; o; o >>= 1) q += __shfl_xor(q, o);
  const float rr = __builtin_amdgcn_rsqf(q * (1.0f / FH) + 1e-5f);
#pragma unroll
  for (int i = 0; i < 3; i++) {
    const int ch = l + 32 * i, P = ch >> 2, jp = ch & 3;
    u32x4 o = {0, 0, 0, 0};
    if (ok[i]) {
#pragma unroll
      for (int e = 0; e < 4; e++) o[e] = cvtpk((bflo(v[i][e]) - mean) * rr, (bfhi(v[i][e]) - mean) * rr);
    }
    *(u32x4*)(ws + WS_Z2N + (size_t)P * PANEL + s * 64 + jp * 16) = o;
  }
}

constexpr int K2_NST = 4, K2_STAGE = 32768, K2_NK = 32;
constexpr int K2_LDS = K2_NST * K2_STAGE;

template <int NPAN>
__device__ __forceinline__ void k2_producer(const u16* A, const char* B, unsigned ldsb, int w, int lane, int rot) {
  const srd_t sa = mksrd(A), sb = mksrd(B);
  const unsigned va = (lane >> 3) * 4096 + (((lane & 7) ^ (lane >> 3)) * 16);
  const unsigned vb = lane * 16;
  const unsigned sao = w * 32 * 4096;
  producer_loop<4 + NPAN, K2_NST, K2_NK>([&](int slot, int step) {
    const unsigned k = (step + rot) & 31;
    const unsigned l = ldsb + slot * K2_STAGE;
#pragma unroll
    for (int i = 0; i < 4; i++) dma(l + w * 4096 + i * 1024, va, sa, sao + i * 32768 + k * 128);
#pragma unroll
    for (int i = 0; i < NPAN; i++) dma(l + 16384 + i * 4096 + w * 1024, vb, sb, i * PANEL + k * 4096 + w * 1024);
  });
}

__global__ __launch_bounds__(512, 2) void k2(const u16* __restrict__ wsr, const u16* __restrict__ wsi, const u16* __restrict__ wsp,
                                             const u16* __restrict__ bsp, char* __restrict__ ws) {
  extern __shared__ __attribute__((aligned(16))) char lds[];
  const unsigned ldsb = (unsigned)(size_t)lds;
  const int lane = threadIdx.x & 63, wave = __builtin_amdgcn_readfirstlane(threadIdx.x >> 6);
  const int xcd = blockIdx.x & 7, idx = blockIdx.x >> 3;
  const int g = lane >> 4, r = lane & 15;
  const int jch = (g & 1) * 2 + (g >> 1);
  const int wm = (wave >> 1) & 1, wn = wave & 1;
  if (xcd < 4) {
    const int im = xcd >> 1, mt = (xcd & 1) * 8 + (idx >> 2), nt = idx & 3;
    if (wave >= 4) {
      k2_producer<4>((im ? wsi : wsr) + (size_t)mt * 128 * S, ws + WS_AB + (size_t)(im * 16 + nt * 4) * PANEL, ldsb, wave - 4, lane, 0);
      return;
    }
    f32x4 acc[4][4];
    const Addr ad = mkaddr<4, 1>(ldsb + wm * 8192, ldsb + 16384, wn, lane);
    consumer_loop<4, 4, 1, K2_NST, K2_NK, K2_STAGE>(acc, ad, NoExtra());
    u16* yp = (u16*)(ws + WS_Y) + (size_t)im * S * D;
#pragma unroll
    for (int m = 0; m < 4; m++) {
      const int t = mt * 128 + wm * 64 + m * 16 + r;
#pragma unroll
      for (int pr = 0; pr < 2; pr++)
        *(u32x4*)(yp + (size_t)t * D + nt * 128 + wn * 64 + pr * 32 + jch * 8) = pack_pair(acc[m][2 * pr], acc[m][2 * pr + 1]);
    }
  } else {
    const int mt = (xcd - 4) * 4 + (idx >> 3), nt = idx & 7;
    if (wave >= 4) {
      k2_producer<3>(wsp + (size_t)mt * 128 * S, ws + WS_Z2N + (size_t)(nt * 3) * PANEL, ldsb, wave - 4, lane, 0);
      return;
    }
    const u16* z1 = (const u16*)(ws + WS_Z1);
    u32x4 zp[4]; u32x2 zs[4]; float bias[4];
    const int fp = (nt * 3 + wn) * 32 + jch * 8 - 16;
    const int fs = (nt * 3 + 2) * 32 + wn * 16 + g * 4 - 16;
#pragma unroll
    for (int m = 0; m < 4; m++) {
      const int t = mt * 128 + wm * 64 + m * 16 + r;
      zp[m] = u32x4{0, 0, 0, 0}; zs[m] = u32x2{0, 0};
      if (fp >= 0 && fp < FH) zp[m] = *(const u32x4*)(z1 + (size_t)t * FH + fp);
      if (fs < FH) zs[m] = *(const u32x2*)(z1 + (size_t)t * FH + fs);
      bias[m] = bflo(bsp[t]);
    }
    f32x4 acc[4][3];
    const Addr ad = mkaddr<3, 1>(ldsb + wm * 8192, ldsb + 16384, wn, lane);
    consumer_loop<4, 3, 1, K2_NST, K2_NK, K2_STAGE>(acc, ad, NoExtra());
    u16* h = (u16*)(ws + WS_H);
#pragma unroll
    for (int m = 0; m < 4; m++) {
      const int t = mt * 128 + wm * 64 + m * 16 + r;
      unsigned x0, x1, y0, y1;
      unswap_pair(zp[m], x0, x1, y0, y1);
      const float b = bias[m];
      f32x4 X = {bflo(x0) * (acc[m][0][0] + b), bfhi(x0) * (acc[m][0][1] + b), bflo(x1) * (acc[m][0][2] + b), bfhi(x1) * (acc[m][0][3] + b)};
      f32x4 Y = {bflo(y0) * (acc[m][1][0] + b), bfhi(y0) * (acc[m][1][1] + b), bflo(y1) * (acc[m][1][2] + b), bfhi(y1) * (acc[m][1][3] + b)};
      const u32x4 v = pack_pair(X, Y);
      if (fp >= 0 && fp < HP) *(u32x4*)(h + (size_t)t * HP + fp) = v;
      const u32x2 u = {cvtpk(bflo(zs[m][0]) * (acc[m][2][0] + b), bfhi(zs[m][0]) * (acc[m][2][1] + b)),
                       cvtpk(bflo(zs[m][1]) * (acc[m][2][2] + b), bfhi(zs[m][1]) * (acc[m][2][3] + b))};
      if (fs < HP) *(u32x2*)(h + (size_t)t * HP + fs) = u;
    }
  }
}

constexpr int K3_NST = 5, K3_STAGE = 16384, K3_NK = 11;
constexpr int K3_LDS = K3_NST * K3_STAGE;

__global__ __launch_bounds__(512, 2) void k3(const u16* __restrict__ x, const u16* __restrict__ wout, const char* __restrict__ ws,
                                             u16* __restrict__ out) {
  extern __shared__ __attribute__((aligned(16))) char lds[];
  const unsigned ldsb = (unsigned)(size_t)lds;
  const int lane = threadIdx.x & 63, wave = __builtin_amdgcn_readfirstlane(threadIdx.x >> 6);
  const int xcd = blockIdx.x & 7, idx = blockIdx.x >> 3;
  const int mt = xcd * 4 + (idx & 3), nt = idx >> 2;
  if (wave >= 4) {
    const int w = wave - 4;
    const srd_t sa = mksrd(ws + WS_H), sb = mksrd(wout);
    const unsigned va = (lane >> 3) * (HP * 2) + (((lane & 7) ^ (lane >> 3)) * 16);
    const int row = lane >> 2, j = (lane & 3) ^ (((row >> 3) & 1) << 1);
    const unsigned vb = row * 1024 + j * 16;
    const unsigned sao = (mt * 64 + w * 16) * (HP * 2);
    const unsigned sbo = nt * 128;
    producer_loop<4, K3_NST, K3_NK>([&](int slot, int step) {
      const unsigned l = ldsb + slot * K3_STAGE;
#pragma unroll
      for (int i = 0; i < 2; i++) dma(l + w * 2048 + i * 1024, va, sa, sao + i * 8 * (HP * 2) + step * 128);
      const int k0 = step * 64 + w * 16;
      const unsigned kr = (k0 >= FH ? FH - 16 : k0) * 1024;
#pragma unroll
      for (int i = 0; i < 2; i++) dma(l + 8192 + i * 4096 + w * 1024, vb, sb, sbo + i * 64 + kr);
    });
    return;
  }
  const int wm = wave >> 1, wn = wave & 1;
  const int g = lane >> 4, r = lane & 15;
  const int jch = (g & 1) * 2 + (g >> 1);
  const u16* yp = (const u16*)(ws + WS_Y);
  u32x4 xv[2], y0v[2], y1v[2];
#pragma unroll
  for (int m = 0; m < 2; m++) {
    const size_t o = (size_t)(mt * 64 + wm * 32 + m * 16 + r) * D + nt * 64 + wn * 32 + jch * 8;
    xv[m] = *(const u32x4*)(x + o);
    y0v[m] = *(const u32x4*)(yp + o);
    y1v[m] = *(const u32x4*)(yp + (size_t)S * D + o);
  }
  f32x4 acc[2][2];
  const Addr ad = mkaddr<2, 1>(ldsb + wm * 4096, ldsb + 8192, wn, lane);
  consumer_loop<2, 2, 1, K3_NST, K3_NK, K3_STAGE>(acc, ad, NoExtra());
#pragma unroll
  for (int m = 0; m < 2; m++) {
    unsigned xa[4], ya[4], yb[4];
    unswap_pair(xv[m], xa[0], xa[1], xa[2], xa[3]);
    unswap_pair(y0v[m], ya[0], ya[1], ya[2], ya[3]);
    unswap_pair(y1v[m], yb[0], yb[1], yb[2], yb[3]);
    f32x4 o[2];
#pragma unroll
    for (int c = 0; c < 2; c++)
#pragma unroll
      for (int e = 0; e < 2; e++) {
        const int i = c * 2 + e;
        o[c][2 * e]     = bflo(xa[i]) + (bflo(ya[i]) - bflo(yb[i])) * silu(acc[m][c][2 * e]);
        o[c][2 * e + 1] = bfhi(xa[i]) + (bfhi(ya[i]) - bfhi(yb[i])) * silu(acc[m][c][2 * e + 1]);
      }
    const size_t oo = (size_t)(mt * 64 + wm * 32 + m * 16 + r) * D + nt * 64 + wn * 32 + jch * 8;
    *(u32x4*)(out + oo) = pack_pair(o[0], o[1]);
  }
}

extern "C" void kernel_launch(void* const* d_in, const int* in_sizes, int n_in, void* d_out, int out_size, void* d_ws,
                              size_t ws_size, hipStream_t stream) {
  const u16* x = (const u16*)d_in[0];
  const u16* wsr = (const u16*)d_in[1];
  const u16* wsi = (const u16*)d_in[2];
  const u16* wdr = (const u16*)d_in[3];
  const u16* wdi = (const u16*)d_in[4];
  const u16* win = (const u16*)d_in[6];
  const u16* wsp = (const u16*)d_in[7];
  const u16* bsp = (const u16*)d_in[8];
  const u16* wout = (const u16*)d_in[9];
  char* ws = (char*)d_ws;
  (void)hipFuncSetAttribute((const void*)k1, hipFuncAttributeMaxDynamicSharedMemorySize, K1_LDS);
  (void)hipFuncSetAttribute((const void*)k2, hipFuncAttributeMaxDynamicSharedMemorySize, K2_LDS);
  (void)hipFuncSetAttribute((const void*)k3, hipFuncAttributeMaxDynamicSharedMemorySize, K3_LDS);
  k1<<<256, 512, K1_LDS, stream>>>(x, wdr, wdi, win, ws);
  k1b<<<256, 256, 0, stream>>>(ws);
  k2<<<256, 512, K2_LDS, stream>>>(wsr, wsi, wsp, bsp, ws);
  k3<<<256, 512, K3_LDS, stream>>>(x, wout, ws, (u16*)d_out);
}
```

```cpp
#include <hip/hip_runtime.h>
#include <hip/hip_bf16.h>

typedef unsigned short u16;
typedef unsigned long long u64;
typedef __attribute__((ext_vector_type(8))) __bf16 bf16x8;
typedef __attribute__((ext_vector_type(2))) __bf16 bf16x2;
typedef __attribute__((ext_vector_type(4))) short s16x4;
typedef __attribute__((ext_vector_type(8))) short s16x8;
typedef __attribute__((ext_vector_type(4))) float f32x4;
typedef __attribute__((ext_vector_type(2))) float f32x2;
typedef __attribute__((ext_vector_type(4))) unsigned u32x4;
typedef __attribute__((ext_vector_type(2))) unsigned u32x2;
#define LDS3 __attribute__((address_space(3)))
typedef __attribute__((address_space(1))) u64 gu64;
typedef __attribute__((address_space(1))) unsigned gu32;
typedef __amdgpu_buffer_rsrc_t srd_t;

constexpr int S = 2048, D = 512, F = 1376, FH = 688;
constexpr unsigned PANEL = S * 64;

constexpr unsigned WS_AB  = 0;
constexpr unsigned WS_Z2N = WS_AB + 32 * PANEL;
constexpr unsigned WS_H   = WS_Z2N + 24 * PANEL;
constexpr unsigned WS_Y   = WS_H + 24 * PANEL;
constexpr unsigned WS_STAT = WS_Y + 128 * 32768;
constexpr unsigned WS_FLAG = WS_STAT + 16 * 8 * 128 * 16;
constexpr unsigned FL_AB = 0, FL_Z2 = 128, FL_H = 384, FL_Y = 512, FL_EPOCH = 640;
constexpr unsigned SPIN_MAX = 1u << 17;
#define PREAT 3
#define MIDAT 4
#define BM2Y 3
#define ROT2Y (nt + mt)
#define PHI2Y (4 * xcd)
#define BM2G 3
#define ROT2G (nt + mt)
#define PHI2G (4 * xcd)

constexpr unsigned SLOT = 32768;
constexpr unsigned L_HR = 90112;
constexpr unsigned L_X  = 155648;
constexpr unsigned X_R = L_X, X_Q = L_X + 1024, X_C = L_X + 2048, X_S1 = L_X + 3328, X_S2 = L_X + 5376, X_W = L_X + 7424;
constexpr int LDS_BYTES = 163840;

__device__ __forceinline__ float bflo(unsigned v) { return __uint_as_float(v << 16); }
__device__ __forceinline__ float bfhi(unsigned v) { return __uint_as_float(v & 0xffff0000u); }
__device__ __forceinline__ unsigned cvtpk(float lo, float hi) {
  return __builtin_bit_cast(unsigned, __builtin_convertvector(f32x2{lo, hi}, bf16x2));
}
__device__ __forceinline__ srd_t mksrd(const void* p, unsigned bytes = 0x7fffffff) {
  return __builtin_amdgcn_make_buffer_rsrc((void*)p, (short)0, bytes, 0x00020000);
}
__device__ __forceinline__ void dma(unsigned m0v, unsigned voff, srd_t srd, unsigned soff) {
  asm volatile("s_mov_b32 m0, %0\n\ts_nop 0\n\tbuffer_load_dwordx4 %1, %2, %3 offen lds"
               :: "s"(m0v), "v"(voff), "s"(srd), "s"(soff) : "memory");
}
__device__ __forceinline__ void dmac(unsigned m0v, unsigned voff, srd_t srd, unsigned soff) {
  asm volatile("s_mov_b32 m0, %0\n\ts_nop 0\n\tbuffer_load_dwordx4 %1, %2, %3 offen sc1 lds"
               :: "s"(m0v), "v"(voff), "s"(srd), "s"(soff) : "memory");
}
template <int N> __device__ __forceinline__ void vmcnt() { asm volatile("s_waitcnt vmcnt(%0)" :: "n"(N) : "memory"); }
__device__ __forceinline__ void lgkm0() { __builtin_amdgcn_s_waitcnt(0xC07F); }
#define BAR() do { __builtin_amdgcn_sched_barrier(0); __builtin_amdgcn_s_barrier(); __builtin_amdgcn_sched_barrier(0); } while (0)

__device__ __forceinline__ void st16(srd_t ws, unsigned off, const u32x4& v) { __builtin_amdgcn_raw_buffer_store_b128(v, ws, off, 0, 16); }
__device__ __forceinline__ void st8(srd_t ws, unsigned off, const u32x2& v) { __builtin_amdgcn_raw_buffer_store_b64(v, ws, off, 0, 16); }
__device__ __forceinline__ u64 flagval(unsigned tag) { return ((u64)(~tag) << 32) | tag; }
__device__ __forceinline__ gu64* flagp(char* ws, unsigned idx) { return (gu64*)(ws + WS_FLAG + (size_t)idx * 128); }
__device__ __forceinline__ void publish(char* ws, unsigned ldsb, int cnt, unsigned flag, unsigned tag, int lane) {
  vmcnt<0>();
  if (lane == 0) {
    const unsigned old = __hip_atomic_fetch_add((LDS3 unsigned*)(size_t)(ldsb + X_W + cnt * 4), 1u, __ATOMIC_RELAXED, __HIP_MEMORY_SCOPE_WORKGROUP);
    if (old == 3) __hip_atomic_store(flagp(ws, flag), flagval(tag), __ATOMIC_RELAXED, __HIP_MEMORY_SCOPE_AGENT);
  }
}
__device__ __forceinline__ void poll(char* ws, unsigned first, unsigned stride, int n, unsigned tag, int lane) {
  const u64 want = flagval(tag);
  gu64* p = flagp(ws, first + (lane < n ? lane : 0) * stride);
  for (unsigned spin = 0; spin < SPIN_MAX; spin++) {
    const u64 v = __hip_atomic_load(p, __ATOMIC_RELAXED, __HIP_MEMORY_SCOPE_AGENT);
    if (__all(v == want)) break;
    __builtin_amdgcn_s_sleep(1);
  }
}
__device__ __forceinline__ void acquire() {
  __builtin_amdgcn_fence(__ATOMIC_ACQUIRE, "wavefront");
  asm volatile("" ::: "memory");
}
__device__ __forceinline__ void prod_wait(char* ws, unsigned ldsb, int word, int w, unsigned first, unsigned stride, int n, unsigned tag, int lane) {
  LDS3 unsigned* lw = (LDS3 unsigned*)(size_t)(ldsb + X_W + word * 4);
  if (w == 0) {
    poll(ws, first, stride, n, tag, lane);
    acquire();
    if (lane == 0) __hip_atomic_store(lw, 1u, __ATOMIC_RELAXED, __HIP_MEMORY_SCOPE_WORKGROUP);
  } else {
    for (unsigned spin = 0; spin < 64 * SPIN_MAX; spin++) {
      if (__builtin_amdgcn_readfirstlane(__hip_atomic_load(lw, __ATOMIC_RELAXED, __HIP_MEMORY_SCOPE_WORKGROUP))) break;
      __builtin_amdgcn_s_sleep(1);
    }
  }
  asm volatile("" ::: "memory");
}

template <int MB, int NB> struct Frags { bf16x8 a[MB]; bf16x8 b[NB]; };
struct Addr { unsigned a[2][2], b0[2][2], b1[2][2], bs[2][2]; };

__device__ __forceinline__ bf16x8 lds128(unsigned addr) { return *(const LDS3 bf16x8*)(size_t)addr; }
__device__ __forceinline__ bf16x8 ldstr(unsigned addr) {
  s16x4 lo = __builtin_bit_cast(s16x4, __builtin_amdgcn_ds_read_tr16_b64_v4i16((LDS3 s16x4*)(size_t)addr));
  s16x4 hi = __builtin_bit_cast(s16x4, __builtin_amdgcn_ds_read_tr16_b64_v4i16((LDS3 s16x4*)(size_t)(addr + 256)));
  s16x8 v = {lo[0], lo[1], lo[2], lo[3], hi[0], hi[1], hi[2], hi[3]};
  return __builtin_bit_cast(bf16x8, v);
}
__device__ __forceinline__ bf16x8 ldstr2(unsigned addr, unsigned addr2) {
  s16x4 lo = __builtin_bit_cast(s16x4, __builtin_amdgcn_ds_read_tr16_b64_v4i16((LDS3 s16x4*)(size_t)addr));
  s16x4 hi = __builtin_bit_cast(s16x4, __builtin_amdgcn_ds_read_tr16_b64_v4i16((LDS3 s16x4*)(size_t)addr2));
  s16x8 v = {lo[0], lo[1], lo[2], lo[3], hi[0], hi[1], hi[2], hi[3]};
  return __builtin_bit_cast(bf16x8, v);
}
template <int NB, int BT, int SLT, int H>
__device__ __forceinline__ void rd(Frags<4, NB>& f, const Addr& ad) {
  constexpr int hi = SLT >> 1;
  constexpr unsigned off = (SLT & 1) * SLOT;
#pragma unroll
  for (int m = 0; m < 4; m++) f.a[m] = lds128(ad.a[H][hi] + off + m * 2048);
  if constexpr (BT == 0) {
#pragma unroll
    for (int c = 0; c < (NB & ~1); c++) f.b[c] = lds128(ad.b0[H][hi] + off + c * 2048);
    if constexpr (NB & 1) f.b[NB - 1] = lds128(ad.bs[H][hi] + off);
  } else {
#pragma unroll
    for (int c = 0; c < (NB & ~1); c++) f.b[c] = ldstr(((c & 1) ? ad.b1[H][hi] : ad.b0[H][hi]) + off + (c >> 1) * 4096);
    if constexpr ((NB & 1) && BT == 1) f.b[NB - 1] = ldstr(ad.bs[H][hi] + off);
    if constexpr ((NB & 1) && BT == 2) f.b[NB - 1] = ldstr2(ad.bs[H][hi] + off, (ad.bs[H][hi] ^ 128) + off);
  }
}
template <int MB, int NB>
__device__ __forceinline__ void mm(f32x4 (&acc)[MB][NB], const Frags<MB, NB>& f) {
#pragma unroll
  for (int m = 0; m < MB; m++)
#pragma unroll
    for (int c = 0; c < NB; c++) acc[m][c] = __builtin_amdgcn_mfma_f32_16x16x32_bf16(f.b[c], f.a[m], acc[m][c], 0, 0, 0);
}
template <int NR, int NM> __device__ __forceinline__ void interleave() {
  constexpr int NI = (NR + 1) / 2;
#pragma unroll
  for (int i = 0; i < NI; i++) { __builtin_amdgcn_sched_group_barrier(0x8, 1, 0); __builtin_amdgcn_sched_group_barrier(0x100, 2, 0); }
  __builtin_amdgcn_sched_group_barrier(0x8, NM - NI, 0);
}
template <int NB, int BT>
__device__ __forceinline__ Addr mkaddr(unsigned abase, unsigned bbase, int wn, int lane, int kf) {
  const int g = lane >> 4, r = lane & 15, q = r >> 2, p = lane & 3;
  constexpr int NPAIR = NB / 2;
  Addr ad;
#pragma unroll
  for (int H = 0; H < 2; H++) {
    const unsigned ks = H ^ kf;
    const unsigned l = (r * 128 + ((g ^ (r & 7)) * 16)) ^ (ks * 64);
    const unsigned a = abase + l;
    unsigned b0, b1, bs;
    if constexpr (BT == 0) {
      b0 = bbase + wn * NPAIR * 4096 + l; b1 = 0;
      bs = bbase + 2 * NPAIR * 4096 + wn * 2048 + l;
    } else {
      const unsigned t = (8 * g + q) * 64 + 8 * (p & 1) + ks * 2048;
      const unsigned c0 = (((p >> 1)) ^ ((g & 1) << 1)) * 16, c1 = ((2 + (p >> 1)) ^ ((g & 1) << 1)) * 16;
      b0 = bbase + wn * NPAIR * 4096 + t + c0;
      b1 = bbase + wn * NPAIR * 4096 + t + c1;
      bs = bbase + 2 * NPAIR * 4096 + t + (wn ? c1 : c0);
      if constexpr (BT == 2) bs = bbase + 2 * NPAIR * 4096 + (ks * 2 + (g >> 1)) * 1024 + wn * 512 + ((g & 1) * 12 + q) * 32 + p * 8;
    }
#pragma unroll
    for (int hi = 0; hi < 2; hi++) {
      ad.a[H][hi] = a + hi * 65536; ad.b0[H][hi] = b0 + hi * 65536; ad.b1[H][hi] = b1 + hi * 65536; ad.bs[H][hi] = bs + hi * 65536;
    }
  }
  return ad;
}
struct NoExtra { template <class Fr> __device__ __forceinline__ void operator()(const Fr&) const {} };

template <int NB, int BT, int NX, int SLT, class Extra>
__device__ __forceinline__ void cstep(f32x4 (&acc)[4][NB], Frags<4, NB>& f0, Frags<4, NB>& f1, const Addr& ad, Extra& extra) {
  constexpr int NR = 4 + (BT ? 2 * NB : NB);
  rd<NB, BT, SLT, 1>(f1, ad);
  mm<4, NB>(acc, f0);
  extra(f0);
  interleave<NR, 4 * NB + NX>();
  __builtin_amdgcn_sched_barrier(0);
  lgkm0();
  BAR();
  rd<NB, BT, (SLT + 1) & 3, 0>(f0, ad);
  mm<4, NB>(acc, f1);
  interleave<NR, 4 * NB>();
  __builtin_amdgcn_sched_barrier(0);
}
struct NoHook { __device__ __forceinline__ void operator()() const {} };
template <int NB, int BT, int NK, int NX, class Extra, class Mid = NoHook, class Pre = NoHook>
__device__ __forceinline__ void consumer_loop(f32x4 (&acc)[4][NB], const Addr& ad, Extra extra, Mid mid = Mid(), Pre pre = Pre()) {
  Frags<4, NB> f0, f1;
#pragma unroll
  for (int m = 0; m < 4; m++)
#pragma unroll
    for (int c = 0; c < NB; c++) acc[m][c] = f32x4{0, 0, 0, 0};
  BAR();
  rd<NB, BT, 0, 0>(f0, ad);
  if constexpr (__is_same(Mid, NoHook)) {
#pragma unroll 1
    for (int i = 0; i < NK / 4; i++) {
      cstep<NB, BT, NX, 0>(acc, f0, f1, ad, extra);
      cstep<NB, BT, NX, 1>(acc, f0, f1, ad, extra);
      cstep<NB, BT, NX, 2>(acc, f0, f1, ad, extra);
      cstep<NB, BT, NX, 3>(acc, f0, f1, ad, extra);
    }
  } else {
    cstep<NB, BT, NX, 0>(acc, f0, f1, ad, extra);
    cstep<NB, BT, NX, 1>(acc, f0, f1, ad, extra);
    if (PREAT == 2) pre();
    cstep<NB, BT, NX, 2>(acc, f0, f1, ad, extra);
    if (PREAT == 3) pre();
    cstep<NB, BT, NX, 3>(acc, f0, f1, ad, extra);
    if (PREAT == 4) pre();
    if (MIDAT == 4) mid();
    cstep<NB, BT, NX, 0>(acc, f0, f1, ad, extra);
    if (PREAT == 5) pre();
    if (MIDAT == 5) mid();
    cstep<NB, BT, NX, 1>(acc, f0, f1, ad, extra);
    if (MIDAT == 6) mid();
    cstep<NB, BT, NX, 2>(acc, f0, f1, ad, extra);
    cstep<NB, BT, NX, 3>(acc, f0, f1, ad, extra);
  }
}
__device__ __forceinline__ u32x4 pack_pair(const f32x4& x, const f32x4& y) {
  unsigned x0 = cvtpk(x[0], x[1]), x1 = cvtpk(x[2], x[3]), y0 = cvtpk(y[0], y[1]), y1 = cvtpk(y[2], y[3]);
  auto s0 = __builtin_amdgcn_permlane16_swap(x0, y0, false, false);
  auto s1 = __builtin_amdgcn_permlane16_swap(x1, y1, false, false);
  return u32x4{s0[0], s1[0], s0[1], s1[1]};
}
__device__ __forceinline__ void unswap_pair(const u32x4& v, unsigned& x0, unsigned& x1, unsigned& y0, unsigned& y1) {
  auto s0 = __builtin_amdgcn_permlane16_swap(v[0], v[2], false, false);
  auto s1 = __builtin_amdgcn_permlane16_swap(v[1], v[3], false, false);
  x0 = s0[0]; y0 = s0[1]; x1 = s1[0]; y1 = s1[1];
}
__device__ __forceinline__ float gsum(float v) {
  const unsigned u = __float_as_uint(v);
  auto a = __builtin_amdgcn_permlane32_swap(u, u, false, false);
  const unsigned t = __float_as_uint(__uint_as_float(a[0]) + __uint_as_float(a[1]));
  auto b = __builtin_amdgcn_permlane16_swap(t, t, false, false);
  return __uint_as_float(b[0]) + __uint_as_float(b[1]);
}
__device__ __forceinline__ float gelu(float v) {
  const float c0 = -2.0f * 0.7978845608028654f * 1.4426950408889634f, c1 = c0 * 0.044715f;
  float e = __builtin_amdgcn_exp2f(v * __builtin_fmaf(v * v, c1, c0));
  return v * __builtin_amdgcn_rcpf(1.0f + e);
}
__device__ __forceinline__ f32x2 gelu2(f32x2 v) {
  const float c0 = -2.0f * 0.7978845608028654f * 1.4426950408889634f, c1 = c0 * 0.044715f;
  const f32x2 q = v * __builtin_elementwise_fma(v * v, f32x2{c1, c1}, f32x2{c0, c0});
  const f32x2 d = f32x2{__builtin_amdgcn_exp2f(q[0]), __builtin_amdgcn_exp2f(q[1])} + 1.0f;
  return v * f32x2{__builtin_amdgcn_rcpf(d[0]), __builtin_amdgcn_rcpf(d[1])};
}
__device__ __forceinline__ f32x2 lo2(const f32x4& v) { return f32x2{v[0], v[1]}; }
__device__ __forceinline__ f32x2 hi2(const f32x4& v) { return f32x2{v[2], v[3]}; }
__device__ __forceinline__ f32x4 cat4(f32x2 a, f32x2 b) { return f32x4{a[0], a[1], b[0], b[1]}; }
__device__ __forceinline__ float silu(float v) {
  float e = __builtin_amdgcn_exp2f(v * -1.4426950408889634f);
  return v * __builtin_amdgcn_rcpf(1.0f + e);
}
__device__ __forceinline__ float keep(float v, unsigned mask) { return __uint_as_float(__float_as_uint(v) & mask); }
#define MFMA(a, b, c) __builtin_amdgcn_mfma_f32_16x16x32_bf16(a, b, c, 0, 0, 0)

template <int NB, int BT, bool ROWS, class Mid = NoHook, class End = NoHook, class Pre = NoHook>
__device__ __forceinline__ void p1_tile(f32x4 (&acc)[4][NB], float (&rr)[4], float (&mr)[4], unsigned ldsb, int wave, int lane,
                                        Mid mid = Mid(), End end = End(), Pre pre = Pre()) {
  const int wm = wave >> 1, wn = wave & 1, g = lane >> 4, r = lane & 15;
  f32x4 rs[4], sq[4], cs[NB];
  const unsigned one2 = 0x3F803F80u;
  const bf16x8 ones = __builtin_bit_cast(bf16x8, u32x4{one2, one2, one2, one2});
#pragma unroll
  for (int m = 0; m < 4; m++) { rs[m] = f32x4{0, 0, 0, 0}; sq[m] = f32x4{0, 0, 0, 0}; }
#pragma unroll
  for (int c = 0; c < NB; c++) cs[c] = f32x4{0, 0, 0, 0};
  const Addr ad = mkaddr<NB, BT>(ldsb + wm * 8192, ldsb + 16384, wn, lane, wm ^ wn);
  consumer_loop<NB, BT, 8, NB + (ROWS ? 8 : 0)>(acc, ad, [&](const Frags<4, NB>& f) {
#pragma unroll
    for (int c = 0; c < NB; c++) cs[c] = MFMA(f.b[c], ones, cs[c]);
    if constexpr (ROWS) {
#pragma unroll
      for (int m = 0; m < 4; m++) { rs[m] = MFMA(ones, f.a[m], rs[m]); sq[m] = MFMA(f.a[m], f.a[m], sq[m]); }
    }
  }, mid, pre);
  end();
  LDS3 float* xr = (LDS3 float*)(size_t)(ldsb + X_R);
  LDS3 float* xq = (LDS3 float*)(size_t)(ldsb + X_Q);
  LDS3 float* xc = (LDS3 float*)(size_t)(ldsb + X_C);
  if constexpr (ROWS) {
#pragma unroll
    for (int m = 0; m < 4; m++) {
      if (g == 0) xr[wave * 64 + m * 16 + r] = rs[m][0];
      const int e = r & 3;
      const float d = e == 0 ? sq[m][0] : e == 1 ? sq[m][1] : e == 2 ? sq[m][2] : sq[m][3];
      if (g == (r >> 2)) xq[wave * 64 + m * 16 + r] = d;
    }
  }
  if (r == 0) {
#pragma unroll
    for (int c = 0; c < NB; c++) *(LDS3 f32x4*)(xc + wave * 80 + c * 16 + g * 4) = cs[c];
  }
  lgkm0();
  BAR();
  if constexpr (ROWS) {
#pragma unroll
    for (int m = 0; m < 4; m++) {
      const int i = m * 16 + r;
      const float sum = xr[wave * 64 + i] + xr[(wave ^ 1) * 64 + i];
      const float ssq = xq[wave * 64 + i] + xq[(wave ^ 1) * 64 + i];
      const float mean = sum * (1.0f / D);
      const float var = fmaxf(ssq * (1.0f / D) - mean * mean, 0.0f);
      rr[m] = __builtin_amdgcn_rsqf(var + 1e-5f);
      mr[m] = mean;
    }
  }
#pragma unroll
  for (int c = 0; c < NB; c++) cs[c] = *(LDS3 f32x4*)(xc + wave * 80 + c * 16 + g * 4) + *(LDS3 f32x4*)(xc + (wave ^ 2) * 80 + c * 16 + g * 4);
#pragma unroll
  for (int m = 0; m < 4; m++) {
    const f32x2 r2 = {rr[m], rr[m]}, n2 = {-mr[m], -mr[m]};
#pragma unroll
    for (int c = 0; c < NB; c++)
      acc[m][c] = cat4(r2 * __builtin_elementwise_fma(n2, lo2(cs[c]), lo2(acc[m][c])), r2 * __builtin_elementwise_fma(n2, hi2(cs[c]), hi2(acc[m][c])));
  }
}

struct F3 { bf16x8 a[4][2]; bf16x8 b[4][2]; };
template <int SS, int NH>
__device__ __forceinline__ void rd3(F3& f, unsigned a0, unsigned a1, unsigned b0, unsigned b1) {
#pragma unroll
  for (int h = 0; h < NH; h++) {
    const unsigned ao = (SS & 3) * 16384 + (h >> 1) * 8192;
#pragma unroll
    for (int m = 0; m < 2; m++) f.a[h][m] = lds128(((h & 1) ? a1 : a0) + ao + m * 2048);
    constexpr int hiB = (SS * 2) >= 8;
    const unsigned bo = (SS * 2 + (h >> 1)) * 8192 + (h & 1) * 2048 - (hiB ? 65536 : 0);
    f.b[h][0] = ldstr(b0 + (hiB ? 65536 : 0) + bo);
    f.b[h][1] = ldstr(b1 + (hiB ? 65536 : 0) + bo);
  }
}
template <int NH>
__device__ __forceinline__ void mm3(f32x4 (&acc)[2][2], const F3& f) {
#pragma unroll
  for (int h = 0; h < NH; h++)
#pragma unroll
    for (int m = 0; m < 2; m++)
#pragma unroll
      for (int c = 0; c < 2; c++) acc[m][c] = MFMA(f.b[h][c], f.a[h][m], acc[m][c]);
}
template <int NR, int NM> __device__ __forceinline__ void interleave3() {
#pragma unroll
  for (int i = 0; i < NM; i++) { __builtin_amdgcn_sched_group_barrier(0x8, 1, 0); __builtin_amdgcn_sched_group_barrier(0x100, (NR + NM - 1) / NM, 0); }
}
__device__ __forceinline__ void p3_consumer(const u16* x, u16* out, char* ws, unsigned ldsb, int wave, int lane, int mt3, int nt3, unsigned tag) {
  const int wm = wave >> 1, wn = wave & 1, g = lane >> 4, r = lane & 15, q = r >> 2, p = lane & 3;
  const int jch = (g & 1) * 2 + (g >> 1);
  u32x4 xv[2], y0v[2], y1v[2];
#pragma unroll
  for (int m = 0; m < 2; m++) xv[m] = *(const u32x4*)(x + (size_t)(mt3 * 64 + wm * 32 + m * 16 + r) * D + nt3 * 64 + wn * 32 + jch * 8);
  const unsigned ytile = (mt3 >> 1) * 4 + (nt3 >> 1);
  const srd_t sws = mksrd(ws);
  const unsigned yp = WS_Y + (ytile * 4 + (mt3 & 1) * 2 + (nt3 & 1)) * 8192 + (wm * 4 + wn) * 1024 + lane * 16;
  poll(ws, FL_Y + ytile, 64, 2, tag, lane);
  acquire();
#pragma unroll
  for (int m = 0; m < 2; m++) { y0v[m] = __builtin_amdgcn_raw_buffer_load_b128(sws, yp + m * 2048, 0, 16); y1v[m] = __builtin_amdgcn_raw_buffer_load_b128(sws, yp + 64 * 32768 + m * 2048, 0, 16); }
  const unsigned l = r * 128 + ((g ^ (r & 7)) * 16);
  const unsigned a0 = ldsb + L_HR + wm * 4096 + l, a1 = ldsb + L_HR + wm * 4096 + (l ^ 64);
  const unsigned t = (8 * g + q) * 64 + 8 * (p & 1);
  const unsigned c0 = (((p >> 1)) ^ ((g & 1) << 1)) * 16, c1 = ((2 + (p >> 1)) ^ ((g & 1) << 1)) * 16;
  const unsigned b0 = ldsb + wn * 4096 + t + c0, b1 = ldsb + wn * 4096 + t + c1;
  f32x4 acc[2][2];
#pragma unroll
  for (int m = 0; m < 2; m++) { acc[m][0] = f32x4{0, 0, 0, 0}; acc[m][1] = f32x4{0, 0, 0, 0}; }
  F3 fa, fb;
  BAR();
  rd3<0, 4>(fa, a0, a1, b0, b1); lgkm0();
  BAR(); rd3<1, 4>(fb, a0, a1, b0, b1); mm3<4>(acc, fa); interleave3<24, 16>(); __builtin_amdgcn_sched_barrier(0); lgkm0();
  BAR(); rd3<2, 4>(fa, a0, a1, b0, b1); mm3<4>(acc, fb); interleave3<24, 16>(); __builtin_amdgcn_sched_barrier(0); lgkm0();
  BAR(); rd3<3, 4>(fb, a0, a1, b0, b1); mm3<4>(acc, fa); interleave3<24, 16>(); __builtin_amdgcn_sched_barrier(0); lgkm0();
  BAR(); rd3<4, 4>(fa, a0, a1, b0, b1); mm3<4>(acc, fb); interleave3<24, 16>(); __builtin_amdgcn_sched_barrier(0); lgkm0();
  BAR(); rd3<5, 2>(fb, a0, a1, b0, b1); mm3<4>(acc, fa); interleave3<12, 16>(); __builtin_amdgcn_sched_barrier(0);
  mm3<2>(acc, fb);
  const srd_t so = mksrd(out, S * D * 2);
#pragma unroll
  for (int m = 0; m < 2; m++) {
    unsigned xa[4];
    unswap_pair(xv[m], xa[0], xa[1], xa[2], xa[3]);
    f32x4 o[2];
#pragma unroll
    for (int c = 0; c < 2; c++)
#pragma unroll
      for (int e = 0; e < 2; e++) {
        const int i = c * 2 + e;
        o[c][2 * e]     = bflo(xa[i]) + (bflo(y0v[m][i]) - bflo(y1v[m][i])) * silu(acc[m][c][2 * e]);
        o[c][2 * e + 1] = bfhi(xa[i]) + (bfhi(y0v[m][i]) - bfhi(y1v[m][i])) * silu(acc[m][c][2 * e + 1]);
      }
    __builtin_amdgcn_raw_buffer_store_b128(pack_pair(o[0], o[1]), so, ((mt3 * 64 + wm * 32 + m * 16 + r) * D + nt3 * 64 + wn * 32 + jch * 8) * 2, 0, 16);
  }
}
__device__ __forceinline__ void p3_producer(char* ws, unsigned ldsb, int w, int lane, int mt3, unsigned tag) {
  const srd_t sh = mksrd(ws + WS_H);
  {
    LDS3 unsigned* lw = (LDS3 unsigned*)(size_t)(ldsb + X_W + 2 * 4);
    for (unsigned spin = 0; spin < 64 * SPIN_MAX; spin++) {
      if (__builtin_amdgcn_readfirstlane(__hip_atomic_load(lw, __ATOMIC_RELAXED, __HIP_MEMORY_SCOPE_WORKGROUP)) == 4) break;
      __builtin_amdgcn_s_sleep(1);
    }
  }
  if (blockIdx.x == 0 && w == 3) {
    bool ok = true;
    for (int i = 0; i < 4; i++) { poll(ws, FL_AB + i * 64, 1, 64, tag, lane); }
    if (ok && lane == 0) __hip_atomic_store((gu32*)(ws + WS_FLAG + FL_EPOCH * 128), tag, __ATOMIC_RELAXED, __HIP_MEMORY_SCOPE_AGENT);
  }
  prod_wait(ws, ldsb, 6, w, FL_H + (mt3 >> 1) * 8, 1, 8, tag, lane);
  const int c = (lane & 7) ^ (lane >> 3);
  unsigned vh[3][2];
#pragma unroll
  for (int i = 0; i < 2; i++) {
    const unsigned row = mt3 * 64 + w * 16 + i * 8 + (lane >> 3), pq = (c >> 2) * PANEL;
    const unsigned plain = row * 64 + (c & 3) * 16 + pq, half = row * 32 + ((c & 3) >> 1) * (PANEL / 2) + (c & 1) * 16 + pq;
    vh[0][i] = plain; vh[1][i] = (c >> 2) ? plain : half; vh[2][i] = (c >> 2) ? half : plain;
  }
  auto H = [&](int s, int nsteps) {
#pragma unroll
    for (int st = 0; st < 2; st++)
      if (st < nsteps) {
#pragma unroll
        for (int i = 0; i < 2; i++)
          dmac(ldsb + L_HR + (s & 3) * 16384 + st * 8192 + w * 2048 + i * 1024, vh[(s * 2 + st) % 3][i], sh, (s * 2 + st) * 2 * PANEL);
      }
  };
  H(0, 2); H(1, 2); H(2, 2); H(3, 2);
  vmcnt<12>(); BAR();
  vmcnt<8>(); BAR(); H(4, 2);
  vmcnt<8>(); BAR(); H(5, 1);
  vmcnt<6>(); BAR();
  vmcnt<2>(); BAR();
  vmcnt<0>(); BAR();
}

struct P1Src { srd_t sx, sw; unsigned vk, vt, sxo; };
__device__ __forceinline__ void issue_x(const P1Src& p, unsigned l, int w, int k) {
#pragma unroll
  for (int i = 0; i < 4; i++) dma(l + w * 4096 + i * 1024, p.vk, p.sx, p.sxo + i * 8192 + k * 128);
}
__device__ __forceinline__ void issue_win(const P1Src& p, srd_t swin, unsigned l, int w, int k, unsigned col0) {
#pragma unroll
  for (int i = 0; i < 3; i++) dma(l + 16384 + i * 4096 + w * 1024, p.vt, swin, col0 * 2 + i * 64 + (k * 64 + w * 16) * (F * 2));
}
template <int BM> __device__ __forceinline__ unsigned kmap(int step, int rot, int phi) {
  return (unsigned)((((step + phi) & 31) & ~BM) | ((step + rot) & BM));
}
template <int NP, int BM>
__device__ __forceinline__ void p2_producer(unsigned boff, char* ws, unsigned ldsb, int w, int lane, int rot,
                                            unsigned flag0, unsigned tag, srd_t sa, unsigned va,
                                            const u16* wout, int nt3, int phi) {
  const srd_t sb = mksrd(ws);
  const unsigned vb = lane * 16, sao = w * 32 * 4096;
  const unsigned vb2 = (lane >> 5) * (PANEL / 2) + (((lane >> 1) & 15) ^ ((lane >> 2) & 4)) * 32 + (lane & 1) * 16;
  auto kk = [&](int step) { return kmap<BM>(step, rot, phi); };
  auto IA = [&](int step) {
    const unsigned l = ldsb + (step & 3) * SLOT, k = kk(step);
#pragma unroll
    for (int i = 0; i < 4; i++) dma(l + w * 4096 + i * 1024, va, sa, sao + i * 32768 + k * 128);
  };
  auto IB = [&](int step) {
    const unsigned l = ldsb + (step & 3) * SLOT, k = kk(step);
#pragma unroll
    for (int i = 0; i < (NP & ~1); i++) dmac(l + 16384 + i * 4096 + w * 1024, vb, sb, boff + i * PANEL + k * 4096 + w * 1024);
    if constexpr (NP & 1) dmac(l + 16384 + (NP - 1) * 4096 + w * 1024, vb2, sb, boff + (NP - 1) * PANEL + k * 2048 + w * 512);
  };
  const srd_t sw = mksrd(wout);
  const int row = lane >> 2, j = (lane & 3) ^ (((row >> 3) & 1) << 1);
  const unsigned vw = row * 1024 + j * 16;
  auto IW = [&](int i0, int i1) {
#pragma unroll
    for (int i = i0; i < i1; i++) {
      const int k0 = i * 64 + w * 16;
      const unsigned kr = (k0 >= FH ? FH - 16 : k0) * 1024;
#pragma unroll
      for (int p = 0; p < 2; p++) dma(ldsb + i * 8192 + p * 4096 + w * 1024, vw, sw, nt3 * 128 + p * 64 + kr);
    }
  };
  prod_wait(ws, ldsb, 4, w, flag0, 1, 16, tag, lane);
  constexpr int N = 4 + NP;
  IB(0); IB(1); IB(2);
  vmcnt<2 * NP>(); BAR(); IA(3); IB(3);
  vmcnt<NP + N>(); BAR(); IA(4); IB(4);
#pragma unroll 1
  for (int p = 2; p < 29; p++) { vmcnt<2 * N>(); BAR(); IA(p + 3); IB(p + 3); }
  vmcnt<2 * N>(); BAR(); IW(0, 4);
  vmcnt<N + 8>(); BAR(); IW(4, 8);
  vmcnt<16>(); BAR(); IW(8, 11);
  BAR();
}

__global__ __launch_bounds__(512, 2) void fused(const u16* __restrict__ x, const u16* __restrict__ wsr, const u16* __restrict__ wsi,
                                                const u16* __restrict__ wdr, const u16* __restrict__ wdi, const u16* __restrict__ win,
                                                const u16* __restrict__ wsp, const u16* __restrict__ bsp, const u16* __restrict__ wout,
                                                char* __restrict__ ws, u16* __restrict__ out) {
  extern __shared__ __attribute__((aligned(16))) char lds[];
  const unsigned ldsb = (unsigned)(size_t)lds;
  const int lane = threadIdx.x & 63, wave = __builtin_amdgcn_readfirstlane(threadIdx.x >> 6);
  const int xcd = blockIdx.x & 7, idx = blockIdx.x >> 3;
  const bool isY = xcd < 4;
  const int im = xcd >> 1;
  const int mt = isY ? (xcd & 1) * 8 + (idx >> 2) : (xcd - 4) * 4 + (idx >> 3);
  const int nt = isY ? (idx & 3) : (idx & 7);
  const int mt3 = xcd * 4 + (idx & 3), nt3 = idx >> 2;
  gu32* const epochp = (gu32*)(ws + WS_FLAG + FL_EPOCH * 128);
#define LOAD_EPOCH() unsigned ep; asm volatile("global_load_dword %0, %1, off sc1" : "=v"(ep) : "v"(epochp) : "memory")
#define GET_TAG(N) ({ asm volatile("s_waitcnt vmcnt(" #N ")" : "+v"(ep) :: "memory"); (unsigned)__builtin_amdgcn_readfirstlane(ep) + 1; })

  if (wave >= 4) {
    const int w = wave - 4;
    unsigned tagp;
    if (w == 0 && lane < 8) *(LDS3 unsigned*)(size_t)(ldsb + X_W + lane * 4) = 0;
    P1Src p;
    p.sx = mksrd(x);
    p.vk = (lane >> 3) * 1024 + (((lane & 7) ^ (lane >> 3)) * 16);
    p.sxo = (mt * 128 + w * 32) * 1024;
    const int row = lane >> 2, jj = (lane & 3) ^ (((row >> 3) & 1) << 1);
    p.vt = row * (F * 2) + jj * 16;
    const srd_t swin = mksrd(win, D * F * 2);
    const unsigned va = (lane >> 3) * 4096 + (((lane & 7) ^ (lane >> 3)) * 16), sao = w * 32 * 4096;
    if (isY) {
      const srd_t sd = mksrd(im ? wdi : wdr);
      const unsigned sdo = (nt * 128 + w * 32) * 1024;
      auto I1a = [&](int n) {
        const unsigned l = ldsb + (n & 3) * SLOT;
        const int k = n;
        issue_x(p, l, w, k);
#pragma unroll
        for (int i = 0; i < 4; i++) dma(l + 16384 + w * 4096 + i * 1024, p.vk, sd, sdo + i * 8192 + k * 128);
      };
      LOAD_EPOCH();
      I1a(0); I1a(1);
      const unsigned tag = GET_TAG(16);
      lgkm0();
#pragma unroll 1
      for (int n = 0; n < 6; n++) { vmcnt<8>(); BAR(); I1a(n + 2); }
      const u16* A = (im ? wsi : wsr) + (size_t)mt * 128 * S;
      const srd_t sa = mksrd(A);
      const int rot = ROT2Y, phi = PHI2Y;
      auto IA = [&](int step) {
        const unsigned l = ldsb + (step & 3) * SLOT, k = kmap<BM2Y>(step, rot, phi);
#pragma unroll
        for (int i = 0; i < 4; i++) dma(l + w * 4096 + i * 1024, va, sa, sao + i * 32768 + k * 128);
      };
      vmcnt<8>(); BAR(); IA(0);
      vmcnt<4>(); BAR(); IA(1);
      BAR(); IA(2);
      BAR();
      p2_producer<4, BM2Y>(WS_AB + (im * 16 + nt * 4) * PANEL, ws, ldsb, w, lane, rot, FL_AB + (im * 4 + nt) * 16, tag, sa, va, wout, nt3, phi);
      tagp = tag;
    } else {
      auto I1 = [&](int n) { const unsigned l = ldsb + (n & 3) * SLOT; const int k = n & 7; issue_x(p, l, w, k); issue_win(p, swin, l, w, k, (n < 8 ? FH : 0) + nt * 96); };
      LOAD_EPOCH();
      I1(0); I1(1);
      const unsigned tag = GET_TAG(14);
      lgkm0();
#pragma unroll 1
      for (int n = 0; n < 8; n++) { vmcnt<7>(); BAR(); I1(n + 2); }
      BAR(); I1(10); I1(11);
      BAR(); BAR();
      vmcnt<21>(); BAR();
#pragma unroll 1
      for (int n = 0; n < 4; n++) { vmcnt<14>(); BAR(); I1(n + 12); }
      const u16* A = wsp + (size_t)mt * 128 * S;
      const srd_t sa = mksrd(A);
      const int rot = ROT2G, phi = PHI2G;
      auto IA = [&](int step) {
        const unsigned l = ldsb + (step & 3) * SLOT, k = kmap<BM2G>(step, rot, phi);
#pragma unroll
        for (int i = 0; i < 4; i++) dma(l + w * 4096 + i * 1024, va, sa, sao + i * 32768 + k * 128);
      };
      vmcnt<14>(); BAR(); IA(0);
      vmcnt<11>(); BAR(); IA(1);
      vmcnt<8>(); BAR(); IA(2);
      BAR(); BAR();
      p2_producer<3, BM2G>(WS_Z2N + nt * 3 * PANEL, ws, ldsb, w, lane, rot, FL_Z2 + nt * 16, tag, sa, va, wout, nt3, phi);
      tagp = tag;
    }
    p3_producer(ws, ldsb, w, lane, mt3, tagp);
    asm volatile("pf_code_end:");
    return;
  }

  const int wm = wave >> 1, wn = wave & 1, g = lane >> 4, r = lane & 15;
  const int jch = (g & 1) * 2 + (g >> 1);
  const unsigned swz = ((r >> 3) & 1) << 1;
  const srd_t sws = mksrd(ws);
  const int t0 = mt * 128 + wm * 64 + r;
  if (wave == 0) {
    unsigned lo, hi, len, dummy;
    asm volatile("s_getpc_b64 s[60:61]\npf_code_begin:\n\ts_mov_b32 %0, s60\n\ts_mov_b32 %1, s61\n\ts_mov_b32 %2, pf_code_end-pf_code_begin"
                 : "=s"(lo), "=s"(hi), "=s"(len) :: "s60", "s61");
    const unsigned off = (lane * 32 + idx) * 64;
    const char* pc = (const char*)(((u64)hi << 32) | lo) + (off < len ? off : 0);
    asm volatile("global_load_dword %0, %1, off" : "=v"(dummy) : "v"(pc) : "memory");
    asm volatile("s_waitcnt vmcnt(0)" : "+v"(dummy) :: "memory");
  }
  const unsigned tag = (unsigned)__builtin_amdgcn_readfirstlane(__hip_atomic_load(epochp, __ATOMIC_RELAXED, __HIP_MEMORY_SCOPE_AGENT)) + 1;
  float rr[4], mr[4];
  if (isY) {
    {
      f32x4 acc[4][4];
      p1_tile<4, 0, true>(acc, rr, mr, ldsb, wave, lane);
#pragma unroll
      for (int m = 0; m < 4; m++)
#pragma unroll
        for (int pr = 0; pr < 2; pr++)
          st16(sws, WS_AB + (im * 16 + nt * 4 + wn * 2 + pr) * PANEL + (t0 + m * 16) * 64 + ((jch ^ swz) * 16), pack_pair(acc[m][2 * pr], acc[m][2 * pr + 1]));
      publish(ws, ldsb, 0, FL_AB + (im * 4 + nt) * 16 + mt, tag, lane);
    }
    {
      f32x4 acc[4][4];
      const Addr ad = mkaddr<4, 1>(ldsb + wm * 8192, ldsb + 16384, wn, lane, 0);
      consumer_loop<4, 1, 32, 0>(acc, ad, NoExtra());
      const unsigned yt = im * 64 + mt * 4 + nt;
#pragma unroll
      for (int m = 0; m < 4; m++)
#pragma unroll
        for (int pr = 0; pr < 2; pr++) {
          const f32x4 &a = acc[m][2 * pr], &b = acc[m][2 * pr + 1];
          st16(sws, WS_Y + (yt * 4 + wave) * 8192 + (m * 2 + pr) * 1024 + lane * 16,
               u32x4{cvtpk(a[0], a[1]), cvtpk(a[2], a[3]), cvtpk(b[0], b[1]), cvtpk(b[2], b[3])});
        }
      publish(ws, ldsb, 2, FL_Y + yt, tag, lane);
    }
  } else {
    unsigned z[24];
    {
      f32x4 acc[4][3];
      p1_tile<3, 1, true>(acc, rr, mr, ldsb, wave, lane);
      const unsigned mk1 = nt < 7 ? ~0u : 0u, mk0 = (nt < 7 || wn == 0) ? ~0u : 0u;
      float s1[4], s2[4];
      unsigned zp[24];
#pragma unroll
      for (int m = 0; m < 4; m++) {
        f32x2 a2 = {0.f, 0.f}, b2 = {0.f, 0.f};
#pragma unroll
        for (int c = 0; c < 3; c++) {
          f32x2 u = gelu2(lo2(acc[m][c])), v = gelu2(hi2(acc[m][c]));
          const unsigned mk = c ? mk1 : mk0;
          u = f32x2{keep(u[0], mk), keep(u[1], mk)}; v = f32x2{keep(v[0], mk), keep(v[1], mk)};
          zp[(m * 3 + c) * 2] = cvtpk(u[0], u[1]); zp[(m * 3 + c) * 2 + 1] = cvtpk(v[0], v[1]);
          a2 += u + v;
          b2 = __builtin_elementwise_fma(u, u, __builtin_elementwise_fma(v, v, b2));
        }
        const float a = a2[0] + a2[1], b = b2[0] + b2[1];
        s1[m] = gsum(a); s2[m] = gsum(b);
      }
      LDS3 f32x2* st1 = (LDS3 f32x2*)(size_t)(ldsb + X_S1);
      LDS3 f32x2* st2 = (LDS3 f32x2*)(size_t)(ldsb + X_S2);
      st1[wave * 64 + lane] = f32x2{g == 0 ? s1[0] : g == 1 ? s1[1] : g == 2 ? s1[2] : s1[3], g == 0 ? s2[0] : g == 1 ? s2[1] : g == 2 ? s2[2] : s2[3]};
      lgkm0();
      BAR();
      const unsigned sbase = WS_STAT + (mt * 8 * 128 + wm * 64 + lane) * 16;
      if (wn == 0) {
        const f32x2 v = st1[wave * 64 + lane] + st1[(wave ^ 1) * 64 + lane];
        st16(sws, sbase + nt * 2048, u32x4{__float_as_uint(v[0]), tag, __float_as_uint(v[1]), tag});
      }
      f32x4 ac1[4][3];
      u32x4 q[8];
      auto ldq = [&]() {
        __builtin_amdgcn_sched_barrier(0);
#pragma unroll
        for (int i = 0; i < 8; i++) q[i] = __builtin_amdgcn_raw_buffer_load_b128(sws, sbase + i * 2048, 0, 16);
        __builtin_amdgcn_sched_barrier(0);
      };
      p1_tile<3, 1, false>(ac1, rr, mr, ldsb, wave, lane, [&]() {
#pragma unroll
        for (int i = 0; i < 24; i++) asm volatile("" : "+v"(zp[i]));
        f32x2 part;
        for (unsigned spin = 0; spin < SPIN_MAX; spin++) {
          bool ok = true;
          part = f32x2{0.f, 0.f};
#pragma unroll
          for (int i = 0; i < 8; i++) { ok = ok && q[i][1] == tag && q[i][3] == tag; part += f32x2{__uint_as_float(q[i][0]), __uint_as_float(q[i][2])}; }
          if (__all(ok)) break;
          __builtin_amdgcn_s_sleep(1);
          asm volatile("" ::: "memory");
          ldq();
        }
        st2[wave * 64 + lane] = part;
        lgkm0();
        f32x4 o2[2];
#pragma unroll
        for (int m = 0; m < 4; m++) {
          const f32x2 v = st2[wave * 64 + m * 16 + r];
          const float mean = v[0] * (1.0f / FH);
          const float var = fmaxf(v[1] * (1.0f / FH) - mean * mean, 0.0f);
          const float rs = __builtin_amdgcn_rsqf(var + 1e-5f);
          const f32x2 r2 = {rs, rs}, n2 = {-mean, -mean};
          f32x4 o[3];
#pragma unroll
          for (int c = 0; c < 3; c++) {
            const unsigned lo = zp[(m * 3 + c) * 2], hi = zp[(m * 3 + c) * 2 + 1];
            const f32x2 u = (f32x2{bflo(lo), bfhi(lo)} + n2) * r2, v = (f32x2{bflo(hi), bfhi(hi)} + n2) * r2;
            const unsigned mk = c ? mk1 : mk0;
            o[c] = f32x4{keep(u[0], mk), keep(u[1], mk), keep(v[0], mk), keep(v[1], mk)};
          }
          o2[m & 1] = o[2];
          const int t = t0 + m * 16;
          st16(sws, WS_Z2N + (nt * 3 + wn) * PANEL + t * 64 + ((jch ^ swz) * 16), pack_pair(o[0], o[1]));
          if (m & 1) st16(sws, WS_Z2N + (nt * 3 + 2) * PANEL + wn * (PANEL / 2) + (t - 16 + (g & 1) * 16) * 32 + (g >> 1) * 16, pack_pair(o2[0], o2[1]));
        }
        lgkm0();
        __builtin_amdgcn_sched_barrier(0);
      }, [&]() {
        __builtin_amdgcn_sched_barrier(0);
        publish(ws, ldsb, 0, FL_Z2 + nt * 16 + mt, tag, lane);
        __builtin_amdgcn_sched_barrier(0);
      }, ldq);
      __builtin_amdgcn_sched_barrier(0);
#pragma unroll
      for (int m = 0; m < 4; m++)
#pragma unroll
        for (int c = 0; c < 3; c++) {
          const f32x2 u = gelu2(lo2(ac1[m][c])), v = gelu2(hi2(ac1[m][c]));
          z[(m * 3 + c) * 2] = cvtpk(u[0], u[1]); z[(m * 3 + c) * 2 + 1] = cvtpk(v[0], v[1]);
        }
#pragma unroll
      for (int i = 0; i < 24; i++) asm volatile("" : "+v"(z[i]));
      __builtin_amdgcn_sched_barrier(0);
    }
    {
      u16 bias[4];
#pragma unroll
      for (int m = 0; m < 4; m++) bias[m] = bsp[t0 + m * 16];
      f32x4 acc[4][3];
      const Addr ad = mkaddr<3, 2>(ldsb + wm * 8192, ldsb + 16384, wn, lane, 0);
      consumer_loop<3, 2, 32, 0>(acc, ad, NoExtra());
      const bool full = nt < 7;
      const unsigned mk1 = nt < 7 ? ~0u : 0u, mk0 = (nt < 7 || wn == 0) ? ~0u : 0u;
#pragma unroll
      for (int m = 0; m < 4; m++) {
        const float b = bflo(bias[m]);
#pragma unroll
        for (int c = 0; c < 3; c++) {
          const unsigned mk = c ? mk1 : mk0;
          const unsigned lo = z[(m * 3 + c) * 2], hi = z[(m * 3 + c) * 2 + 1];
          acc[m][c][0] = keep(bflo(lo) * (acc[m][c][0] + b), mk);
          acc[m][c][1] = keep(bfhi(lo) * (acc[m][c][1] + b), mk);
          acc[m][c][2] = keep(bflo(hi) * (acc[m][c][2] + b), mk);
          acc[m][c][3] = keep(bfhi(hi) * (acc[m][c][3] + b), mk);
        }
        const int t = t0 + m * 16;
        if (full || wn == 0) st16(sws, WS_H + (nt * 3 + wn) * PANEL + t * 64 + jch * 16, pack_pair(acc[m][0], acc[m][1]));
        if ((m & 1) && full) st16(sws, WS_H + (nt * 3 + 2) * PANEL + wn * (PANEL / 2) + (t - 16 + (g & 1) * 16) * 32 + (g >> 1) * 16, pack_pair(acc[m - 1][2], acc[m][2]));
      }
      publish(ws, ldsb, 2, FL_H + mt * 8 + nt, tag, lane);
    }
  }
  p3_consumer(x, out, ws, ldsb, wave, lane, mt3, nt3, tag);
}

extern "C" void kernel_launch(void* const* d_in, const int* in_sizes, int n_in, void* d_out, int out_size, void* d_ws,
                              size_t ws_size, hipStream_t stream) {
  (void)hipFuncSetAttribute((const void*)fused, hipFuncAttributeMaxDynamicSharedMemorySize, LDS_BYTES);
  fused<<<256, 512, LDS_BYTES, stream>>>((const u16*)d_in[0], (const u16*)d_in[1], (const u16*)d_in[2], (const u16*)d_in[3],
                                         (const u16*)d_in[4], (const u16*)d_in[6], (const u16*)d_in[7], (const u16*)d_in[8],
                                         (const u16*)d_in[9], (char*)d_ws, (u16*)d_out);
}
```
